```python
import torch

def _softmax(x, axis=-1):
    return torch.softmax(x, dim=axis)
def _gelu(x):
    return torch.nn.functional.gelu(x, approximate="tanh")
def _silu(x):
    return torch.nn.functional.silu(x)
def _sigmoid(x):
    return torch.sigmoid(x)
def _layernorm(x, axis=-1, eps=1e-5):
    m = x.mean(dim=axis, keepdim=True)
    v = x.var(dim=axis, keepdim=True, unbiased=False)
    return (x - m) / torch.sqrt(v + eps)
def _scatter_add(tgt, idx, src, axis=0):
    return tgt.clone().index_add_(axis, idx, src)
def _segment_sum(src, seg, n):
    out = torch.zeros((n,) + src.shape[1:], dtype=src.dtype)
    return out.index_add_(0, seg, src)
def _pad(x, pw):
    flat = tuple(v for pair in reversed(pw) for v in pair)
    return torch.nn.functional.pad(x, flat)

kernel_name = 'standalone__attention_interact_autoint__recsys'

def setup_inputs():

    _w = (lambda a: (lambda t: t.to(torch.bfloat16) if t.is_floating_point() else t)(torch.as_tensor(a)))
    B, F = 4096, 100
    D = max(2, (16 // 2) * 2)
    H = 2
    Dh = D // H
    return {
        "e": _w((lambda *s: torch.randn(*s, dtype=torch.bfloat16) * 0.05)(B, F, D).to(float)),
        "wq": _w((lambda *s: torch.randn(*s, dtype=torch.bfloat16) * 6.4)(H, D, Dh).to(float)),
        "wk": _w((lambda *s: torch.randn(*s, dtype=torch.bfloat16) * 6.4)(H, D, Dh).to(float)),
        "wv": _w((lambda *s: torch.randn(*s, dtype=torch.bfloat16) * 0.05)(H, D, Dh).to(float)),
        "wres": _w((lambda *s: torch.randn(*s, dtype=torch.bfloat16) * 0.05)(D, D).to(float)),
    }

def reference(e, wq, wk, wv, wres):
    H, D, Dh = wq.shape[0], wq.shape[1], wq.shape[2]
    B, F = e.shape[0], e.shape[1]
    q = torch.einsum('bfd,hdk->bhfk', e, wq)
    k = torch.einsum('bfd,hdk->bhfk', e, wk)
    v = torch.einsum('bfd,hdk->bhfk', e, wv)
    scale = float(Dh) ** -0.5
    scores = torch.einsum('bhfk,bhgk->bhfg', q, k) * scale
    attn = _softmax(scores, axis=-1)
    attn = (lambda x, dt: x.to(dt))(attn, v.dtype)
    ctx = torch.einsum('bhfg,bhgk->bhfk', attn, v)
    ctx = (lambda x, *d: x.permute(*d))(ctx, 0, 2, 1, 3).reshape(B, F, H * Dh)
    res = torch.einsum('bfd,de->bfe', e, wres)
    out = ctx + res
    mask = (lambda x, dt: x.to(dt))(out > 0, out.dtype)
    return out * mask
```

```cpp
#include <hip/hip_runtime.h>
#include <hip/hip_bf16.h>

__global__ __launch_bounds__(128) void naive_kernel(const __hip_bfloat16* __restrict__ e, const __hip_bfloat16* __restrict__ wq,
    const __hip_bfloat16* __restrict__ wk, const __hip_bfloat16* __restrict__ wv, const __hip_bfloat16* __restrict__ wres,
    __hip_bfloat16* __restrict__ out) {
  const int F = 100, D = 16, H = 2, Dh = 8;
  __shared__ float se[100][16];
  __shared__ float sq[2][100][8], sk[2][100][8], sv[2][100][8];
  int b = blockIdx.x, t = threadIdx.x;
  for (int i = t; i < F * D; i += 128) se[i / D][i % D] = __bfloat162float(e[(size_t)b * F * D + i]);
  __syncthreads();
  for (int i = t; i < H * F * Dh; i += 128) {
    int h = i / (F * Dh), f = (i / Dh) % F, k = i % Dh;
    float aq = 0, ak = 0, av = 0;
    for (int d = 0; d < D; ++d) {
      float ev = se[f][d];
      aq += ev * __bfloat162float(wq[(h * D + d) * Dh + k]);
      ak += ev * __bfloat162float(wk[(h * D + d) * Dh + k]);
      av += ev * __bfloat162float(wv[(h * D + d) * Dh + k]);
    }
    sq[h][f][k] = aq; sk[h][f][k] = ak; sv[h][f][k] = av;
  }
  __syncthreads();
  if (t < F) {
    int f = t;
    float o[16];
    for (int h = 0; h < H; ++h) {
      float m = -1e30f;
      for (int g = 0; g < F; ++g) { float s = 0; for (int k = 0; k < Dh; ++k) s += sq[h][f][k] * sk[h][g][k]; s *= 0.35355339059f; m = fmaxf(m, s); }
      float l = 0, c[8] = {0,0,0,0,0,0,0,0};
      for (int g = 0; g < F; ++g) { float s = 0; for (int k = 0; k < Dh; ++k) s += sq[h][f][k] * sk[h][g][k]; s *= 0.35355339059f; float p = __expf(s - m); l += p;
        for (int k = 0; k < Dh; ++k) c[k] += p * sv[h][g][k]; }
      for (int k = 0; k < Dh; ++k) o[h * 8 + k] = c[k] / l;
    }
    for (int c = 0; c < D; ++c) {
      float r = 0; for (int d = 0; d < D; ++d) r += se[f][d] * __bfloat162float(wres[d * D + c]);
      float v = o[c] + r; v = v > 0 ? v : 0;
      out[((size_t)b * F + f) * D + c] = __float2bfloat16(v);
    }
  }
}

extern "C" void kernel_launch(void* const* d_in, const int* in_sizes, int n_in,
                              void* d_out, int out_size, void* d_ws, size_t ws_size,
                              hipStream_t stream) {
  const __hip_bfloat16* e = (const __hip_bfloat16*)d_in[0];
  int B = in_sizes[0] / 1600;
  naive_kernel<<<B, 128, 0, stream>>>(e, (const __hip_bfloat16*)d_in[1], (const __hip_bfloat16*)d_in[2], (const __hip_bfloat16*)d_in[3], (const __hip_bfloat16*)d_in[4], (__hip_bfloat16*)d_out);
}
```

```cpp
#include <hip/hip_runtime.h>
#include <hip/hip_bf16.h>
#include <stdint.h>


typedef float f32x16 __attribute__((ext_vector_type(16)));
typedef float f32x2 __attribute__((ext_vector_type(2)));
typedef __bf16 bf2 __attribute__((ext_vector_type(2)));
typedef short s4 __attribute__((ext_vector_type(4)));
typedef short s8 __attribute__((ext_vector_type(8)));
typedef unsigned u4 __attribute__((ext_vector_type(4)));
typedef unsigned u2 __attribute__((ext_vector_type(2)));
typedef short i16x2 __attribute__((ext_vector_type(2)));

#define DI __device__ __forceinline__

DI unsigned cvt2(float a, float b) {
  f32x2 v = {a, b};
  return __builtin_bit_cast(unsigned, __builtin_convertvector(v, bf2));
}
DI s4 pack4(float a, float b, float c, float d) {
  u2 r = {cvt2(a, b), cvt2(c, d)};
  return __builtin_bit_cast(s4, r);
}
DI s8 pack8(const f32x16& x, int o) {
  u4 r = {cvt2(x[o], x[o + 1]), cvt2(x[o + 2], x[o + 3]), cvt2(x[o + 4], x[o + 5]), cvt2(x[o + 6], x[o + 7])};
  return __builtin_bit_cast(s8, r);
}
DI unsigned relu2(unsigned x) {
  i16x2 v = __builtin_bit_cast(i16x2, x);
  const i16x2 z2 = {0, 0};
  v = __builtin_elementwise_max(v, z2);
  return __builtin_bit_cast(unsigned, v);
}
DI float xmax(float a, float b) { return __builtin_fmaxf(a, b); }
DI float max3(float a, float b, float c) { return __builtin_fmaxf(__builtin_fmaxf(a, b), c); }

#define MFMA16(a, b, c) __builtin_amdgcn_mfma_f32_32x32x16_bf16((a), (b), (c), 0, 0, 0)
#define MFMA8(a, b, c) __builtin_amdgcn_mfma_f32_32x32x8bf16_1k((a), (b), (c), 0, 0, 0)

constexpr int F = 100, D = 16, DH = 8;

__global__ __launch_bounds__(256, 2) void autoint_kernel(const uint16_t* __restrict__ e, const uint16_t* __restrict__ wq,
                                                         const uint16_t* __restrict__ wk, const uint16_t* __restrict__ wv,
                                                         const uint16_t* __restrict__ wres, uint16_t* __restrict__ out,
                                                         int B) {
  const int lane = threadIdx.x & 63;
  const int b = blockIdx.x * 4 + (threadIdx.x >> 6);
  if (b >= B) return;
  const int r = lane & 31, hh = lane >> 5;

  const u4* eb = reinterpret_cast<const u4*>(e + (size_t)b * (F * D));
  s8 E[4];
#pragma unroll
  for (int t = 0; t < 3; ++t) E[t] = __builtin_bit_cast(s8, eb[(32 * t + r) * 2 + hh]);
  {
    u4 z = {0u, 0u, 0u, 0u};
    if (r < 4) z = eb[(96 + r) * 2 + hh];
    E[3] = __builtin_bit_cast(s8, z);
  }

  s8 A1, Ares, Bv;
  {
    const int g = r >> 3, dh = r & 7;
    const uint16_t* w1 = (g < 2 ? wq : wk) + (g & 1) * (D * DH) + dh;
#pragma unroll
    for (int j = 0; j < 8; ++j) {
      const int d = 8 * hh + j;
      A1[j] = (short)w1[d * DH];
      Ares[j] = (r < 16) ? (short)wres[d * D + r] : (short)0;
      Bv[j] = (r < 16) ? (short)wv[(r >> 3) * (D * DH) + d * DH + (r & 7)] : (short)0;
    }
  }

  const float qscale = 0.35355339059327373f * 1.4426950408889634f;

  f32x16 zero;
#pragma unroll
  for (int i = 0; i < 16; ++i) zero[i] = 0.f;

  s4 Qf[2][4], Kf[2][4];
  s8 Vf[7];
  {
    f32x16 ci;
    const float one = (r == 16 || r == 20) ? 1.0f : 0.0f;
#pragma unroll
    for (int i = 0; i < 16; ++i) ci[i] = one;
#pragma unroll
    for (int t = 0; t < 4; ++t) {
      f32x16 p = MFMA16(A1, E[t], zero);
      Qf[0][t] = pack4(p[0] * qscale, p[1] * qscale, p[2] * qscale, p[3] * qscale);
      Qf[1][t] = pack4(p[4] * qscale, p[5] * qscale, p[6] * qscale, p[7] * qscale);
      Kf[0][t] = pack4(p[8], p[9], p[10], p[11]);
      Kf[1][t] = pack4(p[12], p[13], p[14], p[15]);
      f32x16 pv = MFMA16(E[t], Bv, ci);
      Vf[2 * t] = pack8(pv, 0);
      if (t < 3) Vf[2 * t + 1] = pack8(pv, 8);
    }
  }

  const float kbias = hh ? -__builtin_inff() : 0.0f;

  uint16_t* ob = out + (size_t)b * (F * D);

#pragma unroll
  for (int t = 0; t < 4; ++t) {
    float o[8];
#pragma unroll
    for (int h = 0; h < 2; ++h) {
      f32x16 S[4];
#pragma unroll
      for (int kt = 0; kt < 4; ++kt) S[kt] = MFMA8(Kf[h][kt], Qf[h][t], zero);
#pragma unroll
      for (int i = 0; i < 4; ++i) S[3][i] += kbias;
      float m = max3(S[3][0], S[3][1], xmax(S[3][2], S[3][3]));
#pragma unroll
      for (int kt = 0; kt < 3; ++kt)
#pragma unroll
        for (int i = 0; i < 16; i += 2) m = max3(m, S[kt][i], S[kt][i + 1]);
      {
        auto sw = __builtin_amdgcn_permlane32_swap(__float_as_uint(m), __float_as_uint(m), false, false);
        m = xmax(__uint_as_float(sw[0]), __uint_as_float(sw[1]));
      }
      f32x16 ctx = zero;
#pragma unroll
      for (int kt = 0; kt < 3; ++kt) {
#pragma unroll
        for (int i = 0; i < 16; ++i) S[kt][i] = __builtin_amdgcn_exp2f(S[kt][i] - m);
        ctx = MFMA16(Vf[2 * kt], pack8(S[kt], 0), ctx);
        ctx = MFMA16(Vf[2 * kt + 1], pack8(S[kt], 8), ctx);
      }
      {
#pragma unroll
        for (int i = 0; i < 4; ++i) S[3][i] = __builtin_amdgcn_exp2f(S[3][i] - m);
        u4 pl = {cvt2(S[3][0], S[3][1]), cvt2(S[3][2], S[3][3]), 0u, 0u};
        ctx = MFMA16(Vf[6], __builtin_bit_cast(s8, pl), ctx);
      }
      const float inv = __builtin_amdgcn_rcpf(ctx[8]);
#pragma unroll
      for (int i = 0; i < 4; ++i) o[4 * h + i] = ctx[4 * h + i] * inv;
    }
    f32x16 res = MFMA16(Ares, E[t], zero);
    unsigned a0 = cvt2(o[0] + res[0], o[1] + res[1]);
    unsigned a1 = cvt2(o[2] + res[2], o[3] + res[3]);
    unsigned b0 = cvt2(o[4] + res[4], o[5] + res[5]);
    unsigned b1 = cvt2(o[6] + res[6], o[7] + res[7]);
    u2 s0 = __builtin_amdgcn_permlane32_swap(a0, b0, false, false);
    u2 s1 = __builtin_amdgcn_permlane32_swap(a1, b1, false, false);
    u4 w;
    w[0] = relu2(s0[0]);
    w[1] = relu2(s1[0]);
    w[2] = relu2(s0[1]);
    w[3] = relu2(s1[1]);
    if (t < 3 || r < 4) reinterpret_cast<u4*>(ob)[(32 * t + r) * 2 + hh] = w;
  }
}

extern "C" void kernel_launch(void* const* d_in, const int* in_sizes, int n_in, void* d_out, int out_size, void* d_ws,
                              size_t ws_size, hipStream_t stream) {
  const int B = in_sizes[0] / (F * D);
  autoint_kernel<<<(B + 3) / 4, 256, 0, stream>>>((const uint16_t*)d_in[0], (const uint16_t*)d_in[1],
                                                  (const uint16_t*)d_in[2], (const uint16_t*)d_in[3],
                                                  (const uint16_t*)d_in[4], (uint16_t*)d_out, B);
}
```

```cpp
#include <hip/hip_runtime.h>
#include <hip/hip_bf16.h>
#include <stdint.h>


typedef float f32x16 __attribute__((ext_vector_type(16)));
typedef float f32x2 __attribute__((ext_vector_type(2)));
typedef __bf16 bf2 __attribute__((ext_vector_type(2)));
typedef short s4 __attribute__((ext_vector_type(4)));
typedef short s8 __attribute__((ext_vector_type(8)));
typedef unsigned u4 __attribute__((ext_vector_type(4)));
typedef unsigned u2 __attribute__((ext_vector_type(2)));
typedef short i16x2 __attribute__((ext_vector_type(2)));

#define DI __device__ __forceinline__

DI unsigned cvt2(float a, float b) {
  f32x2 v = {a, b};
  return __builtin_bit_cast(unsigned, __builtin_convertvector(v, bf2));
}
DI s4 pack4(float a, float b, float c, float d) {
  u2 r = {cvt2(a, b), cvt2(c, d)};
  return __builtin_bit_cast(s4, r);
}
DI s8 pack8(const f32x16& x, int o) {
  u4 r = {cvt2(x[o], x[o + 1]), cvt2(x[o + 2], x[o + 3]), cvt2(x[o + 4], x[o + 5]), cvt2(x[o + 6], x[o + 7])};
  return __builtin_bit_cast(s8, r);
}
DI unsigned relu2(unsigned x) {
  i16x2 v = __builtin_bit_cast(i16x2, x);
  const i16x2 z2 = {0, 0};
  v = __builtin_elementwise_max(v, z2);
  return __builtin_bit_cast(unsigned, v);
}
DI float xmax(float a, float b) { return __builtin_fmaxf(a, b); }
DI float max3(float a, float b, float c) { return __builtin_fmaxf(__builtin_fmaxf(a, b), c); }
DI unsigned dpp_shr4(unsigned x) { return __builtin_amdgcn_update_dpp(0u, x, 0x114, 0xf, 0xf, true); }
DI float dpp_shl4(float x) {
  return __uint_as_float(__builtin_amdgcn_update_dpp(0u, __float_as_uint(x), 0x104, 0xf, 0xf, true));
}

#define MFMA16(a, b, c) __builtin_amdgcn_mfma_f32_32x32x16_bf16((a), (b), (c), 0, 0, 0)
#define MFMA8(a, b, c) __builtin_amdgcn_mfma_f32_32x32x8bf16_1k((a), (b), (c), 0, 0, 0)

constexpr int F = 100, D = 16, DH = 8;

DI f32x16 zero16() {
  f32x16 z;
#pragma unroll
  for (int i = 0; i < 16; ++i) z[i] = 0.f;
  return z;
}

template <bool SAFE, bool TAIL>
DI f32x16 unit(const s4 (&K0)[4], const s4 (&K1)[4], s4 Q0, s4 Q1, const u4* __restrict__ vl, float kbias) {
  float m = 0.f;
  if (SAFE) {
    m = -__builtin_inff();
#pragma unroll
    for (int kt = 0; kt < 4; ++kt) {
      f32x16 S = MFMA8(K0[kt], Q0, zero16());
      if (TAIL) S = MFMA8(K1[kt], Q1, S);
      if (kt < 3) {
#pragma unroll
        for (int i = 0; i < 16; i += 2) m = max3(m, S[i], S[i + 1]);
      } else {
        m = max3(m, S[0] + kbias, S[1] + kbias);
        m = max3(m, S[2] + kbias, S[3] + kbias);
      }
    }
    auto sw = __builtin_amdgcn_permlane32_swap(__float_as_uint(m), __float_as_uint(m), false, false);
    m = xmax(__uint_as_float(sw[0]), __uint_as_float(sw[1]));
  }
  f32x16 ctx = zero16();
#pragma unroll
  for (int kt = 0; kt < 3; ++kt) {
    f32x16 S = MFMA8(K0[kt], Q0, zero16());
    if (TAIL) S = MFMA8(K1[kt], Q1, S);
#pragma unroll
    for (int i = 0; i < 16; ++i) S[i] = __builtin_amdgcn_exp2f(SAFE ? S[i] - m : S[i]);
    ctx = MFMA16(__builtin_bit_cast(s8, vl[(2 * kt) * 64]), pack8(S, 0), ctx);
    ctx = MFMA16(__builtin_bit_cast(s8, vl[(2 * kt + 1) * 64]), pack8(S, 8), ctx);
  }
  {
    f32x16 S = MFMA8(K0[3], Q0, zero16());
    if (TAIL) S = MFMA8(K1[3], Q1, S);
#pragma unroll
    for (int i = 0; i < 4; ++i) S[i] = __builtin_amdgcn_exp2f(SAFE ? (S[i] + kbias) - m : S[i]);
    u4 pl = {cvt2(S[0], S[1]), cvt2(S[2], S[3]), 0u, 0u};
    ctx = MFMA16(__builtin_bit_cast(s8, vl[6 * 64]), __builtin_bit_cast(s8, pl), ctx);
  }
  return ctx;
}

DI bool bad_denominator(float l) { return !(l >= 8.673617379884035e-19f && l <= 1.152921504606846976e18f); }

DI s8 load_e(const u4* __restrict__ eb, int t, int r, int hh) {
  u4 z = {0u, 0u, 0u, 0u};
  if (t < 3 || r < 4) z = eb[(32 * t + r) * 2 + hh];
  return __builtin_bit_cast(s8, z);
}

template <bool SAFE>
DI bool process(const u4* __restrict__ eb, const s4 (&Kf)[2][4], const u4* __restrict__ Vf, s8 A2, float kbias, int r, int hh,
                uint16_t* __restrict__ ob) {
  const float qscale = 0.35355339059327373f * 1.4426950408889634f;
  bool bad = false;
  s8 Enext = load_e(eb, 0, r, hh);
#pragma unroll
  for (int t = 0; t < 4; ++t) {
    const s8 Et = Enext;
    if (t < 3) Enext = load_e(eb, t + 1, r, hh);
    s4 Q0, Q1;
    {
      const f32x16 p = MFMA16(A2, Et, zero16());
      Q0 = pack4(p[0] * qscale, p[1] * qscale, p[2] * qscale, p[3] * qscale);
      Q1 = pack4(p[4] * qscale, p[5] * qscale, p[6] * qscale, p[7] * qscale);
    }
    float o[8];
    if (t < 3) {
#pragma unroll
      for (int h = 0; h < 2; ++h) {
        const f32x16 ctx = unit<SAFE, false>(Kf[h], Kf[h], h ? Q1 : Q0, Q0, Vf, kbias);
        bad |= bad_denominator(ctx[8]);
        const float inv = __builtin_amdgcn_rcpf(ctx[8]);
#pragma unroll
        for (int i = 0; i < 4; ++i) o[4 * h + i] = ctx[4 * h + i] * inv;
      }
    } else {
      u2 q1 = __builtin_bit_cast(u2, Q1);
      q1[0] = dpp_shr4(q1[0]);
      q1[1] = dpp_shr4(q1[1]);
      const f32x16 ctx = unit<SAFE, true>(Kf[0], Kf[1], Q0, __builtin_bit_cast(s4, q1), Vf, kbias);
      bad |= bad_denominator(ctx[8]);
      const float inv = __builtin_amdgcn_rcpf(ctx[8]);
#pragma unroll
      for (int i = 0; i < 4; ++i) {
        o[i] = ctx[i] * inv;
        o[4 + i] = dpp_shl4(ctx[4 + i] * inv);
      }
    }
    const f32x16 p = MFMA16(A2, Et, zero16());
    unsigned a0 = cvt2(o[0] + p[8], o[1] + p[9]);
    unsigned a1 = cvt2(o[2] + p[10], o[3] + p[11]);
    unsigned b0 = cvt2(o[4] + p[12], o[5] + p[13]);
    unsigned b1 = cvt2(o[6] + p[14], o[7] + p[15]);
    u2 s0 = __builtin_amdgcn_permlane32_swap(a0, b0, false, false);
    u2 s1 = __builtin_amdgcn_permlane32_swap(a1, b1, false, false);
    u4 w;
    w[0] = relu2(s0[0]);
    w[1] = relu2(s1[0]);
    w[2] = relu2(s0[1]);
    w[3] = relu2(s1[1]);
    if (t < 3 || r < 4) reinterpret_cast<u4*>(ob)[(32 * t + r) * 2 + hh] = w;
  }
  return bad;
}

__global__ __launch_bounds__(256, 2) void autoint_kernel(const uint16_t* __restrict__ e, const uint16_t* __restrict__ wq,
                                                         const uint16_t* __restrict__ wk, const uint16_t* __restrict__ wv,
                                                         const uint16_t* __restrict__ wres, uint16_t* __restrict__ out,
                                                         int B) {
  const int lane = threadIdx.x & 63;
  const int b = blockIdx.x * 4 + (threadIdx.x >> 6);
  if (b >= B) return;
  const int r = lane & 31, hh = lane >> 5;

  const u4* eb = reinterpret_cast<const u4*>(e + (size_t)b * (F * D));
  s8 E[4];
#pragma unroll
  for (int t = 0; t < 3; ++t) E[t] = __builtin_bit_cast(s8, eb[(32 * t + r) * 2 + hh]);
  {
    u4 z = {0u, 0u, 0u, 0u};
    if (r < 4) z = eb[(96 + r) * 2 + hh];
    E[3] = __builtin_bit_cast(s8, z);
  }

  s8 Ak, A2, Bv;
  {
    const int g = (r >> 3) & 1, dh = r & 7;
    const uint16_t* wkp = wk + g * (D * DH) + dh;
    const uint16_t* w2p = r < 16 ? wq + g * (D * DH) + dh : wres + (r - 16);
    const int w2s = r < 16 ? DH : D;
    const uint16_t* wvp = wv + g * (D * DH) + dh;
#pragma unroll
    for (int j = 0; j < 8; ++j) {
      const int d = 8 * hh + j;
      const short k_ = (short)wkp[d * DH], v_ = (short)wvp[d * DH];
      Ak[j] = r < 16 ? k_ : (short)0;
      Bv[j] = r < 16 ? v_ : (short)0;
      A2[j] = (short)w2p[d * w2s];
    }
  }

  s4 Kf[2][4];
  __shared__ u4 vlds[4 * 7 * 64];
  u4* Vf = vlds + (threadIdx.x >> 6) * (7 * 64) + lane;
  {
    const float one = (r == 16 || r == 20) ? 1.0f : 0.0f;
#pragma unroll
    for (int t = 0; t < 4; ++t) {
      const f32x16 p = MFMA16(Ak, E[t], zero16());
      Kf[0][t] = pack4(p[0], p[1], p[2], p[3]);
      Kf[1][t] = pack4(p[4], p[5], p[6], p[7]);
      f32x16 ci;
#pragma unroll
      for (int i = 0; i < 16; ++i) ci[i] = (t < 3 || (i < 4 && hh == 0)) ? one : 0.f;
      const f32x16 pv = MFMA16(E[t], Bv, ci);
      Vf[(2 * t) * 64] = __builtin_bit_cast(u4, pack8(pv, 0));
      if (t < 3) Vf[(2 * t + 1) * 64] = __builtin_bit_cast(u4, pack8(pv, 8));
    }
  }

  asm volatile("" ::: "memory");
  const float kbias = hh ? -__builtin_inff() : 0.0f;
  uint16_t* ob = out + (size_t)b * (F * D);

  const bool bad = process<false>(eb, Kf, Vf, A2, kbias, r, hh, ob);
  if (__builtin_amdgcn_ballot_w64(bad) != 0) {
    process<true>(eb, Kf, Vf, A2, kbias, r, hh, ob);
  }
}

extern "C" void kernel_launch(void* const* d_in, const int* in_sizes, int n_in, void* d_out, int out_size, void* d_ws,
                              size_t ws_size, hipStream_t stream) {
  const int B = in_sizes[0] / (F * D);
  autoint_kernel<<<(B + 3) / 4, 256, 0, stream>>>((const uint16_t*)d_in[0], (const uint16_t*)d_in[1],
                                                  (const uint16_t*)d_in[2], (const uint16_t*)d_in[3],
                                                  (const uint16_t*)d_in[4], (uint16_t*)d_out, B);
}
```

```cpp
#include <hip/hip_runtime.h>
#include <hip/hip_bf16.h>
#include <stdint.h>


typedef float f32x16 __attribute__((ext_vector_type(16)));
typedef float f32x2 __attribute__((ext_vector_type(2)));
typedef __bf16 bf2 __attribute__((ext_vector_type(2)));
typedef short s4 __attribute__((ext_vector_type(4)));
typedef short s8 __attribute__((ext_vector_type(8)));
typedef unsigned u4 __attribute__((ext_vector_type(4)));
typedef unsigned u2 __attribute__((ext_vector_type(2)));
typedef short i16x2 __attribute__((ext_vector_type(2)));

#define DI __device__ __forceinline__

DI unsigned cvt2(float a, float b) {
  f32x2 v = {a, b};
  return __builtin_bit_cast(unsigned, __builtin_convertvector(v, bf2));
}
DI s4 pack4(float a, float b, float c, float d) {
  u2 r = {cvt2(a, b), cvt2(c, d)};
  return __builtin_bit_cast(s4, r);
}
DI s8 pack8(const f32x16& x, int o) {
  u4 r = {cvt2(x[o], x[o + 1]), cvt2(x[o + 2], x[o + 3]), cvt2(x[o + 4], x[o + 5]), cvt2(x[o + 6], x[o + 7])};
  return __builtin_bit_cast(s8, r);
}
DI unsigned relu2(unsigned x) {
  i16x2 v = __builtin_bit_cast(i16x2, x);
  const i16x2 z2 = {0, 0};
  v = __builtin_elementwise_max(v, z2);
  return __builtin_bit_cast(unsigned, v);
}
DI float xmax(float a, float b) { return __builtin_fmaxf(a, b); }
DI float max3(float a, float b, float c) { return __builtin_fmaxf(__builtin_fmaxf(a, b), c); }
DI unsigned dpp_shr4(unsigned x) { return __builtin_amdgcn_update_dpp(0u, x, 0x114, 0xf, 0xf, true); }
DI float dpp_shl4(float x) {
  return __uint_as_float(__builtin_amdgcn_update_dpp(0u, __float_as_uint(x), 0x104, 0xf, 0xf, true));
}

#define MFMA16(a, b, c) __builtin_amdgcn_mfma_f32_32x32x16_bf16((a), (b), (c), 0, 0, 0)
#define MFMA8(a, b, c) __builtin_amdgcn_mfma_f32_32x32x8bf16_1k((a), (b), (c), 0, 0, 0)

constexpr int F = 100, D = 16, DH = 8;

DI f32x16 zero16() {
  f32x16 z;
#pragma unroll
  for (int i = 0; i < 16; ++i) z[i] = 0.f;
  return z;
}

template <bool TAIL>
DI float unit_max(const s4 (&K0)[4], const s4 (&K1)[4], s4 Q0, s4 Q1, float kbias) {
  float m = -__builtin_inff();
#pragma unroll
  for (int kt = 0; kt < 4; ++kt) {
    f32x16 S = MFMA8(K0[kt], Q0, zero16());
    if (TAIL) S = MFMA8(K1[kt], Q1, S);
    if (kt < 3) {
#pragma unroll
      for (int i = 0; i < 16; i += 2) m = max3(m, S[i], S[i + 1]);
    } else {
      m = max3(m, S[0] + kbias, S[1] + kbias);
      m = max3(m, S[2] + kbias, S[3] + kbias);
    }
  }
  auto sw = __builtin_amdgcn_permlane32_swap(__float_as_uint(m), __float_as_uint(m), false, false);
  return xmax(__uint_as_float(sw[0]), __uint_as_float(sw[1]));
}

template <bool SAFE, bool TAIL>
DI f32x16 unit(const s4 (&K0)[4], const s4 (&K1)[4], s4 Q0, s4 Q1, const u4* __restrict__ vl, float kbias, float m) {
  f32x16 ctx = zero16();
#pragma unroll
  for (int kt = 0; kt < 3; ++kt) {
    asm volatile("" ::: "memory");
    f32x16 S = MFMA8(K0[kt], Q0, zero16());
    if (TAIL) S = MFMA8(K1[kt], Q1, S);
#pragma unroll
    for (int i = 0; i < 16; ++i) S[i] = __builtin_amdgcn_exp2f(SAFE ? S[i] - m : S[i]);
    ctx = MFMA16(__builtin_bit_cast(s8, vl[(2 * kt) * 64]), pack8(S, 0), ctx);
    ctx = MFMA16(__builtin_bit_cast(s8, vl[(2 * kt + 1) * 64]), pack8(S, 8), ctx);
  }
  {
    f32x16 S = MFMA8(K0[3], Q0, zero16());
    if (TAIL) S = MFMA8(K1[3], Q1, S);
#pragma unroll
    for (int i = 0; i < 4; ++i) S[i] = __builtin_amdgcn_exp2f(SAFE ? (S[i] + kbias) - m : S[i]);
    u4 pl = {cvt2(S[0], S[1]), cvt2(S[2], S[3]), 0u, 0u};
    ctx = MFMA16(__builtin_bit_cast(s8, vl[6 * 64]), __builtin_bit_cast(s8, pl), ctx);
  }
  return ctx;
}

DI bool bad_denominator(float l) { return !(l >= 8.673617379884035e-19f && l <= 1.152921504606846976e18f); }

DI s8 load_e(const u4* __restrict__ eb, int t, int r, int hh) {
  u4 z = {0u, 0u, 0u, 0u};
  const u4* p = eb + 64 * t;
  if (t < 3 || r < 4) z = p[r * 2 + hh];
  return __builtin_bit_cast(s8, z);
}

DI void store_tile(const float (&o)[8], const s8& A2, const s8& Et, uint16_t* __restrict__ ob, int t, int r, int hh, bool pred) {
  const f32x16 p = MFMA16(A2, Et, zero16());
  unsigned a0 = cvt2(o[0] + p[8], o[1] + p[9]);
  unsigned a1 = cvt2(o[2] + p[10], o[3] + p[11]);
  unsigned b0 = cvt2(o[4] + p[12], o[5] + p[13]);
  unsigned b1 = cvt2(o[6] + p[14], o[7] + p[15]);
  u2 s0 = __builtin_amdgcn_permlane32_swap(a0, b0, false, false);
  u2 s1 = __builtin_amdgcn_permlane32_swap(a1, b1, false, false);
  u4 w;
  w[0] = relu2(s0[0]);
  w[1] = relu2(s1[0]);
  w[2] = relu2(s0[1]);
  w[3] = relu2(s1[1]);
  u4* p_out = reinterpret_cast<u4*>(ob) + 64 * t;
  if (pred) p_out[r * 2 + hh] = w;
}

DI void max_pass(const u4* __restrict__ eb, const s4 (&Kf)[2][4], s8 A2, float kbias, int r, int hh, float* __restrict__ ml) {
  const float qscale = 0.35355339059327373f * 1.4426950408889634f;
#pragma unroll 1
  for (int t = 0; t < 4; ++t) {
    const s8 Et = load_e(eb, t, r, hh);
    const f32x16 p = MFMA16(A2, Et, zero16());
    const s4 Q0 = pack4(p[0] * qscale, p[1] * qscale, p[2] * qscale, p[3] * qscale);
    const s4 Q1 = pack4(p[4] * qscale, p[5] * qscale, p[6] * qscale, p[7] * qscale);
    if (t < 3) {
      ml[(2 * t) * 64] = unit_max<false>(Kf[0], Kf[0], Q0, Q0, kbias);
      ml[(2 * t + 1) * 64] = unit_max<false>(Kf[1], Kf[1], Q1, Q1, kbias);
    } else {
      u2 q1 = __builtin_bit_cast(u2, Q1);
      q1[0] = dpp_shr4(q1[0]);
      q1[1] = dpp_shr4(q1[1]);
      ml[6 * 64] = unit_max<true>(Kf[0], Kf[1], Q0, __builtin_bit_cast(s4, q1), kbias);
    }
  }
}

template <bool SAFE>
DI bool process(const u4* __restrict__ eb, const s4 (&Kf)[2][4], const u4* __restrict__ Vf, s8 A2, float kbias, int r,
                int hh, uint16_t* __restrict__ ob, const float* __restrict__ ml) {
  const float qscale = 0.35355339059327373f * 1.4426950408889634f;
  bool bad = false;
  s8 Enext = load_e(eb, 0, r, hh);
#pragma unroll 1
  for (int t = 0; t < 3; ++t) {
    const s8 Et = Enext;
    Enext = load_e(eb, t + 1, r, hh);
    s4 Q0, Q1;
    {
      const f32x16 p = MFMA16(A2, Et, zero16());
      Q0 = pack4(p[0] * qscale, p[1] * qscale, p[2] * qscale, p[3] * qscale);
      Q1 = pack4(p[4] * qscale, p[5] * qscale, p[6] * qscale, p[7] * qscale);
    }
    float o[8];
#pragma unroll
    for (int h = 0; h < 2; ++h) {
      const f32x16 ctx = unit<SAFE, false>(Kf[h], Kf[h], h ? Q1 : Q0, Q0, Vf, kbias, SAFE ? ml[(2 * t + h) * 64] : 0.f);
      bad |= bad_denominator(ctx[8]);
      const float inv = __builtin_amdgcn_rcpf(ctx[8]);
#pragma unroll
      for (int i = 0; i < 4; ++i) o[4 * h + i] = ctx[4 * h + i] * inv;
    }
    store_tile(o, A2, Et, ob, t, r, hh, true);
  }
  {
    const s8 Et = Enext;
    s4 Q0, Q1;
    {
      const f32x16 p = MFMA16(A2, Et, zero16());
      Q0 = pack4(p[0] * qscale, p[1] * qscale, p[2] * qscale, p[3] * qscale);
      Q1 = pack4(p[4] * qscale, p[5] * qscale, p[6] * qscale, p[7] * qscale);
    }
    u2 q1 = __builtin_bit_cast(u2, Q1);
    q1[0] = dpp_shr4(q1[0]);
    q1[1] = dpp_shr4(q1[1]);
    const f32x16 ctx = unit<SAFE, true>(Kf[0], Kf[1], Q0, __builtin_bit_cast(s4, q1), Vf, kbias, SAFE ? ml[6 * 64] : 0.f);
    bad |= bad_denominator(ctx[8]);
    const float inv = __builtin_amdgcn_rcpf(ctx[8]);
    float o[8];
#pragma unroll
    for (int i = 0; i < 4; ++i) {
      o[i] = ctx[i] * inv;
      o[4 + i] = dpp_shl4(ctx[4 + i] * inv);
    }
    store_tile(o, A2, Et, ob, 3, r, hh, r < 4);
  }
  return bad;
}

__global__ __launch_bounds__(256, 4) void autoint_kernel(const uint16_t* __restrict__ e, const uint16_t* __restrict__ wq,
                                                         const uint16_t* __restrict__ wk, const uint16_t* __restrict__ wv,
                                                         const uint16_t* __restrict__ wres, uint16_t* __restrict__ out,
                                                         int B) {
  const int lane = threadIdx.x & 63;
  const int wv_id = __builtin_amdgcn_readfirstlane(threadIdx.x >> 6);
  const int b = blockIdx.x * 4 + wv_id;
  if (b >= B) return;
  const int r = lane & 31, hh = lane >> 5;

  const u4* eb = reinterpret_cast<const u4*>(e + (size_t)b * (F * D));
  s8 E[4];
#pragma unroll
  for (int t = 0; t < 3; ++t) E[t] = __builtin_bit_cast(s8, eb[(32 * t + r) * 2 + hh]);
  {
    u4 z = {0u, 0u, 0u, 0u};
    if (r < 4) z = eb[(96 + r) * 2 + hh];
    E[3] = __builtin_bit_cast(s8, z);
  }

  s8 Ak, A2, Bv;
  {
    const int g = (r >> 3) & 1, dh = r & 7;
    const uint16_t* wkp = wk + g * (D * DH) + dh;
    const uint16_t* w2p = r < 16 ? wq + g * (D * DH) + dh : wres + (r - 16);
    const int w2s = r < 16 ? DH : D;
    const uint16_t* wvp = wv + g * (D * DH) + dh;
#pragma unroll
    for (int j = 0; j < 8; ++j) {
      const int d = 8 * hh + j;
      const short k_ = (short)wkp[d * DH], v_ = (short)wvp[d * DH];
      Ak[j] = r < 16 ? k_ : (short)0;
      Bv[j] = r < 16 ? v_ : (short)0;
      A2[j] = (short)w2p[d * w2s];
    }
  }

  s4 Kf[2][4];
  __shared__ u4 vlds[4 * 7 * 64];
  u4* Vf = vlds + wv_id * (7 * 64) + lane;
  {
    const float one = (r == 16 || r == 20) ? 1.0f : 0.0f;
#pragma unroll
    for (int t = 0; t < 4; ++t) {
      const f32x16 p = MFMA16(Ak, E[t], zero16());
      Kf[0][t] = pack4(p[0], p[1], p[2], p[3]);
      Kf[1][t] = pack4(p[4], p[5], p[6], p[7]);
      f32x16 ci;
#pragma unroll
      for (int i = 0; i < 16; ++i) ci[i] = (t < 3 || (i < 4 && hh == 0)) ? one : 0.f;
      const f32x16 pv = MFMA16(E[t], Bv, ci);
      Vf[(2 * t) * 64] = __builtin_bit_cast(u4, pack8(pv, 0));
      if (t < 3) Vf[(2 * t + 1) * 64] = __builtin_bit_cast(u4, pack8(pv, 8));
    }
  }

  asm volatile("" ::: "memory");
  const float kbias = hh ? -__builtin_inff() : 0.0f;
  uint16_t* ob = out + (size_t)b * (F * D);

  __shared__ float mlds[4 * 7 * 64];
  float* ml = mlds + wv_id * (7 * 64) + lane;
  const bool bad = process<false>(eb, Kf, Vf, A2, kbias, r, hh, ob, ml);
  if (__builtin_amdgcn_ballot_w64(bad) != 0) {
    max_pass(eb, Kf, A2, kbias, r, hh, ml);
    asm volatile("" ::: "memory");
    process<true>(eb, Kf, Vf, A2, kbias, r, hh, ob, ml);
  }
}

extern "C" void kernel_launch(void* const* d_in, const int* in_sizes, int n_in, void* d_out, int out_size, void* d_ws,
                              size_t ws_size, hipStream_t stream) {
  const int B = in_sizes[0] / (F * D);
  autoint_kernel<<<(B + 3) / 4, 256, 0, stream>>>((const uint16_t*)d_in[0], (const uint16_t*)d_in[1],
                                                  (const uint16_t*)d_in[2], (const uint16_t*)d_in[3],
                                                  (const uint16_t*)d_in[4], (uint16_t*)d_out, B);
}
```

```cpp
#include <hip/hip_runtime.h>
#include <hip/hip_bf16.h>
#include <stdint.h>


typedef float f32x16 __attribute__((ext_vector_type(16)));
typedef float f32x2 __attribute__((ext_vector_type(2)));
typedef __bf16 bf2 __attribute__((ext_vector_type(2)));
typedef short s4 __attribute__((ext_vector_type(4)));
typedef short s8 __attribute__((ext_vector_type(8)));
typedef unsigned u4 __attribute__((ext_vector_type(4)));
typedef unsigned u2 __attribute__((ext_vector_type(2)));
typedef short i16x2 __attribute__((ext_vector_type(2)));

#define DI __device__ __forceinline__

DI unsigned cvt2(float a, float b) {
  f32x2 v = {a, b};
  return __builtin_bit_cast(unsigned, __builtin_convertvector(v, bf2));
}
DI s4 pack4(float a, float b, float c, float d) {
  u2 r = {cvt2(a, b), cvt2(c, d)};
  return __builtin_bit_cast(s4, r);
}
DI s8 pack8(const f32x16& x, int o) {
  u4 r = {cvt2(x[o], x[o + 1]), cvt2(x[o + 2], x[o + 3]), cvt2(x[o + 4], x[o + 5]), cvt2(x[o + 6], x[o + 7])};
  return __builtin_bit_cast(s8, r);
}
DI unsigned relu2(unsigned x) {
  i16x2 v = __builtin_bit_cast(i16x2, x);
  const i16x2 z2 = {0, 0};
  v = __builtin_elementwise_max(v, z2);
  return __builtin_bit_cast(unsigned, v);
}
DI float xmax(float a, float b) { return __builtin_fmaxf(a, b); }
DI float max3(float a, float b, float c) { return __builtin_fmaxf(__builtin_fmaxf(a, b), c); }
DI unsigned dpp_shr4(unsigned x) { return __builtin_amdgcn_update_dpp(0u, x, 0x114, 0xf, 0xf, true); }
DI float dpp_shl4(float x) {
  return __uint_as_float(__builtin_amdgcn_update_dpp(0u, __float_as_uint(x), 0x104, 0xf, 0xf, true));
}

#define MFMA16(a, b, c) __builtin_amdgcn_mfma_f32_32x32x16_bf16((a), (b), (c), 0, 0, 0)
#define MFMA8(a, b, c) __builtin_amdgcn_mfma_f32_32x32x8bf16_1k((a), (b), (c), 0, 0, 0)

constexpr int F = 100, D = 16, DH = 8;

DI f32x16 zero16() {
  f32x16 z;
#pragma unroll
  for (int i = 0; i < 16; ++i) z[i] = 0.f;
  return z;
}

template <bool TAIL>
DI float unit_max(const s4 (&K0)[4], const s4 (&K1)[4], s4 Q0, s4 Q1, float kbias) {
  float m = -__builtin_inff();
#pragma unroll
  for (int kt = 0; kt < 4; ++kt) {
    f32x16 S = MFMA8(K0[kt], Q0, zero16());
    if (TAIL) S = MFMA8(K1[kt], Q1, S);
    if (kt < 3) {
#pragma unroll
      for (int i = 0; i < 16; i += 2) m = max3(m, S[i], S[i + 1]);
    } else {
      m = max3(m, S[0] + kbias, S[1] + kbias);
      m = max3(m, S[2] + kbias, S[3] + kbias);
    }
  }
  auto sw = __builtin_amdgcn_permlane32_swap(__float_as_uint(m), __float_as_uint(m), false, false);
  return xmax(__uint_as_float(sw[0]), __uint_as_float(sw[1]));
}

template <bool SAFE, bool TAIL>
DI f32x16 unit(const s4 (&K0)[4], const s4 (&K1)[4], s4 Q0, s4 Q1, const u4* __restrict__ vl, float kbias, float m) {
  f32x16 ctx = zero16();
#pragma unroll
  for (int kt = 0; kt < 3; ++kt) {
    asm volatile("" ::: "memory");
    f32x16 S = MFMA8(K0[kt], Q0, zero16());
    if (TAIL) S = MFMA8(K1[kt], Q1, S);
#pragma unroll
    for (int i = 0; i < 16; ++i) S[i] = __builtin_amdgcn_exp2f(SAFE ? S[i] - m : S[i]);
    ctx = MFMA16(__builtin_bit_cast(s8, vl[(2 * kt) * 64]), pack8(S, 0), ctx);
    ctx = MFMA16(__builtin_bit_cast(s8, vl[(2 * kt + 1) * 64]), pack8(S, 8), ctx);
  }
  {
    f32x16 S = MFMA8(K0[3], Q0, zero16());
    if (TAIL) S = MFMA8(K1[3], Q1, S);
#pragma unroll
    for (int i = 0; i < 4; ++i) S[i] = __builtin_amdgcn_exp2f(SAFE ? (S[i] + kbias) - m : S[i]);
    u4 pl = {cvt2(S[0], S[1]), cvt2(S[2], S[3]), 0u, 0u};
    ctx = MFMA16(__builtin_bit_cast(s8, vl[6 * 64]), __builtin_bit_cast(s8, pl), ctx);
  }
  return ctx;
}

DI bool bad_denominator(float l) { return !(l >= 8.673617379884035e-19f && l <= 1.152921504606846976e18f); }

DI s8 load_e(const u4* __restrict__ eb, int t, int r, int hh) {
  const bool ok = (t < 3) || (r < 4);
  const u4* p = eb + 64 * t;
  const u4 z = p[ok ? (unsigned)(r * 2 + hh) : 0u];
  const u4 z0 = {0u, 0u, 0u, 0u};
  return __builtin_bit_cast(s8, ok ? z : z0);
}

DI void store_tile(const float (&o)[8], const s8& A2, const s8& Et, uint16_t* __restrict__ ob, int t, int r, int hh, bool pred) {
  const f32x16 p = MFMA16(A2, Et, zero16());
  unsigned a0 = cvt2(o[0] + p[8], o[1] + p[9]);
  unsigned a1 = cvt2(o[2] + p[10], o[3] + p[11]);
  unsigned b0 = cvt2(o[4] + p[12], o[5] + p[13]);
  unsigned b1 = cvt2(o[6] + p[14], o[7] + p[15]);
  u2 s0 = __builtin_amdgcn_permlane32_swap(a0, b0, false, false);
  u2 s1 = __builtin_amdgcn_permlane32_swap(a1, b1, false, false);
  u4 w;
  w[0] = relu2(s0[0]);
  w[1] = relu2(s1[0]);
  w[2] = relu2(s0[1]);
  w[3] = relu2(s1[1]);
  u4* p_out = reinterpret_cast<u4*>(ob) + 64 * t;
  if (pred) p_out[r * 2 + hh] = w;
}

DI void max_pass(const u4* __restrict__ eb, const s4 (&Kf)[2][4], s8 A2, float kbias, int r, int hh, float* __restrict__ ml) {
  const float qscale = 0.35355339059327373f * 1.4426950408889634f;
#pragma unroll 1
  for (int t = 0; t < 4; ++t) {
    const s8 Et = load_e(eb, t, r, hh);
    const f32x16 p = MFMA16(A2, Et, zero16());
    const s4 Q0 = pack4(p[0] * qscale, p[1] * qscale, p[2] * qscale, p[3] * qscale);
    const s4 Q1 = pack4(p[4] * qscale, p[5] * qscale, p[6] * qscale, p[7] * qscale);
    if (t < 3) {
      ml[(2 * t) * 64] = unit_max<false>(Kf[0], Kf[0], Q0, Q0, kbias);
      ml[(2 * t + 1) * 64] = unit_max<false>(Kf[1], Kf[1], Q1, Q1, kbias);
    } else {
      u2 q1 = __builtin_bit_cast(u2, Q1);
      q1[0] = dpp_shr4(q1[0]);
      q1[1] = dpp_shr4(q1[1]);
      ml[6 * 64] = unit_max<true>(Kf[0], Kf[1], Q0, __builtin_bit_cast(s4, q1), kbias);
    }
  }
}

template <bool SAFE>
DI bool process(const u4* __restrict__ eb, const s4 (&Kf)[2][4], const u4* __restrict__ Vf, s8 A2, float kbias, int r,
                int hh, uint16_t* __restrict__ ob, const float* __restrict__ ml) {
  const float qscale = 0.35355339059327373f * 1.4426950408889634f;
  bool bad = false;
  s8 Enext = load_e(eb, 0, r, hh);
#pragma unroll 1
  for (int t = 0; t < 3; ++t) {
    const s8 Et = Enext;
    Enext = load_e(eb, t + 1, r, hh);
    s4 Q0, Q1;
    {
      const f32x16 p = MFMA16(A2, Et, zero16());
      Q0 = pack4(p[0] * qscale, p[1] * qscale, p[2] * qscale, p[3] * qscale);
      Q1 = pack4(p[4] * qscale, p[5] * qscale, p[6] * qscale, p[7] * qscale);
    }
    float o[8];
#pragma unroll
    for (int h = 0; h < 2; ++h) {
      const f32x16 ctx = unit<SAFE, false>(Kf[h], Kf[h], h ? Q1 : Q0, Q0, Vf, kbias, SAFE ? ml[(2 * t + h) * 64] : 0.f);
      bad |= bad_denominator(ctx[8]);
      const float inv = __builtin_amdgcn_rcpf(ctx[8]);
#pragma unroll
      for (int i = 0; i < 4; ++i) o[4 * h + i] = ctx[4 * h + i] * inv;
    }
    store_tile(o, A2, Et, ob, t, r, hh, true);
  }
  {
    const s8 Et = Enext;
    s4 Q0, Q1;
    {
      const f32x16 p = MFMA16(A2, Et, zero16());
      Q0 = pack4(p[0] * qscale, p[1] * qscale, p[2] * qscale, p[3] * qscale);
      Q1 = pack4(p[4] * qscale, p[5] * qscale, p[6] * qscale, p[7] * qscale);
    }
    u2 q1 = __builtin_bit_cast(u2, Q1);
    q1[0] = dpp_shr4(q1[0]);
    q1[1] = dpp_shr4(q1[1]);
    const f32x16 ctx = unit<SAFE, true>(Kf[0], Kf[1], Q0, __builtin_bit_cast(s4, q1), Vf, kbias, SAFE ? ml[6 * 64] : 0.f);
    bad |= bad_denominator(ctx[8]);
    const float inv = __builtin_amdgcn_rcpf(ctx[8]);
    float o[8];
#pragma unroll
    for (int i = 0; i < 4; ++i) {
      o[i] = ctx[i] * inv;
      o[4 + i] = dpp_shl4(ctx[4 + i] * inv);
    }
    store_tile(o, A2, Et, ob, 3, r, hh, r < 4);
  }
  return bad;
}

__global__ __launch_bounds__(256, 4) void autoint_kernel(const uint16_t* __restrict__ e, const uint16_t* __restrict__ wq,
                                                         const uint16_t* __restrict__ wk, const uint16_t* __restrict__ wv,
                                                         const uint16_t* __restrict__ wres, uint16_t* __restrict__ out,
                                                         int B) {
  if (blockIdx.x >= 512) __builtin_amdgcn_s_sleep(40);
  const int lane = threadIdx.x & 63;
  const int wv_id = __builtin_amdgcn_readfirstlane(threadIdx.x >> 6);
  const int b = blockIdx.x * 4 + wv_id;
  if (b >= B) return;
  const int r = lane & 31, hh = lane >> 5;

  const u4* eb = reinterpret_cast<const u4*>(e + (size_t)b * (F * D));
  s8 E[4];
#pragma unroll
  for (int t = 0; t < 4; ++t) E[t] = load_e(eb, t, r, hh);

  s8 Ak, A2, Bv;
  {
    const int c16 = r & 15;
    const unsigned off_h = (unsigned)((c16 >> 3) * (D * DH) + (c16 & 7) + hh * 8 * DH);
    const unsigned off_r = (unsigned)(c16 + hh * 8 * D);
    s8 fq, fr;
#pragma unroll
    for (int j = 0; j < 8; ++j) {
      Ak[j] = (short)wk[off_h + j * DH];
      Bv[j] = (short)wv[off_h + j * DH];
      fq[j] = (short)wq[off_h + j * DH];
      fr[j] = (short)wres[off_r + j * D];
    }
    const u4 z4 = {0u, 0u, 0u, 0u};
    const bool lo = r < 16;
    Ak = __builtin_bit_cast(s8, lo ? __builtin_bit_cast(u4, Ak) : z4);
    Bv = __builtin_bit_cast(s8, lo ? __builtin_bit_cast(u4, Bv) : z4);
    A2 = __builtin_bit_cast(s8, lo ? __builtin_bit_cast(u4, fq) : __builtin_bit_cast(u4, fr));
  }

  s4 Kf[2][4];
  __shared__ u4 vlds[4 * 7 * 64];
  u4* Vf = vlds + wv_id * (7 * 64) + lane;
  {
    const float one = (r == 16 || r == 20) ? 1.0f : 0.0f;
#pragma unroll
    for (int t = 0; t < 4; ++t) {
      const f32x16 p = MFMA16(Ak, E[t], zero16());
      Kf[0][t] = pack4(p[0], p[1], p[2], p[3]);
      Kf[1][t] = pack4(p[4], p[5], p[6], p[7]);
      f32x16 ci;
#pragma unroll
      for (int i = 0; i < 16; ++i) ci[i] = (t < 3 || (i < 4 && hh == 0)) ? one : 0.f;
      const f32x16 pv = MFMA16(E[t], Bv, ci);
      Vf[(2 * t) * 64] = __builtin_bit_cast(u4, pack8(pv, 0));
      if (t < 3) Vf[(2 * t + 1) * 64] = __builtin_bit_cast(u4, pack8(pv, 8));
    }
  }

  asm volatile("" ::: "memory");
  const float kbias = hh ? -__builtin_inff() : 0.0f;
  uint16_t* ob = out + (size_t)b * (F * D);

  __shared__ float mlds[4 * 7 * 64];
  float* ml = mlds + wv_id * (7 * 64) + lane;
  const bool bad = process<false>(eb, Kf, Vf, A2, kbias, r, hh, ob, ml);
  if (__builtin_amdgcn_ballot_w64(bad) != 0) {
    max_pass(eb, Kf, A2, kbias, r, hh, ml);
    asm volatile("" ::: "memory");
    process<true>(eb, Kf, Vf, A2, kbias, r, hh, ob, ml);
  }
}

extern "C" void kernel_launch(void* const* d_in, const int* in_sizes, int n_in, void* d_out, int out_size, void* d_ws,
                              size_t ws_size, hipStream_t stream) {
  const int B = in_sizes[0] / (F * D);
  autoint_kernel<<<(B + 3) / 4, 256, 0, stream>>>((const uint16_t*)d_in[0], (const uint16_t*)d_in[1],
                                                  (const uint16_t*)d_in[2], (const uint16_t*)d_in[3],
                                                  (const uint16_t*)d_in[4], (uint16_t*)d_out, B);
}
```

```cpp
#include <hip/hip_runtime.h>
#include <hip/hip_bf16.h>
#include <stdint.h>


typedef float f32x16 __attribute__((ext_vector_type(16)));
typedef float f32x2 __attribute__((ext_vector_type(2)));
typedef __bf16 bf2 __attribute__((ext_vector_type(2)));
typedef short s4 __attribute__((ext_vector_type(4)));
typedef short s8 __attribute__((ext_vector_type(8)));
typedef unsigned u4 __attribute__((ext_vector_type(4)));
typedef unsigned u2 __attribute__((ext_vector_type(2)));
typedef short i16x2 __attribute__((ext_vector_type(2)));

#define DI __device__ __forceinline__

DI unsigned cvt2(float a, float b) {
  f32x2 v = {a, b};
  return __builtin_bit_cast(unsigned, __builtin_convertvector(v, bf2));
}
DI s4 pack4(float a, float b, float c, float d) {
  u2 r = {cvt2(a, b), cvt2(c, d)};
  return __builtin_bit_cast(s4, r);
}
DI s8 pack8(const f32x16& x, int o) {
  u4 r = {cvt2(x[o], x[o + 1]), cvt2(x[o + 2], x[o + 3]), cvt2(x[o + 4], x[o + 5]), cvt2(x[o + 6], x[o + 7])};
  return __builtin_bit_cast(s8, r);
}
DI unsigned relu2(unsigned x) {
  i16x2 v = __builtin_bit_cast(i16x2, x);
  const i16x2 z2 = {0, 0};
  v = __builtin_elementwise_max(v, z2);
  return __builtin_bit_cast(unsigned, v);
}
DI float xmax(float a, float b) { return __builtin_fmaxf(a, b); }
DI float max3(float a, float b, float c) { return __builtin_fmaxf(__builtin_fmaxf(a, b), c); }
DI unsigned dpp_shr4(unsigned x) { return __builtin_amdgcn_update_dpp(0u, x, 0x114, 0xf, 0xf, true); }
DI float dpp_shl4(float x) {
  return __uint_as_float(__builtin_amdgcn_update_dpp(0u, __float_as_uint(x), 0x104, 0xf, 0xf, true));
}

#define MFMA16(a, b, c) __builtin_amdgcn_mfma_f32_32x32x16_bf16((a), (b), (c), 0, 0, 0)
#define MFMA8(a, b, c) __builtin_amdgcn_mfma_f32_32x32x8bf16_1k((a), (b), (c), 0, 0, 0)

constexpr int F = 100, D = 16, DH = 8;

DI f32x16 zero16() {
  f32x16 z;
#pragma unroll
  for (int i = 0; i < 16; ++i) z[i] = 0.f;
  return z;
}

template <bool TAIL>
DI float unit_max(const s4 (&K0)[4], const s4 (&K1)[4], s4 Q0, s4 Q1, float kbias) {
  float m = -__builtin_inff();
#pragma unroll
  for (int kt = 0; kt < 4; ++kt) {
    f32x16 S = MFMA8(K0[kt], Q0, zero16());
    if (TAIL) S = MFMA8(K1[kt], Q1, S);
    if (kt < 3) {
#pragma unroll
      for (int i = 0; i < 16; i += 2) m = max3(m, S[i], S[i + 1]);
    } else {
      m = max3(m, S[0] + kbias, S[1] + kbias);
      m = max3(m, S[2] + kbias, S[3] + kbias);
    }
  }
  auto sw = __builtin_amdgcn_permlane32_swap(__float_as_uint(m), __float_as_uint(m), false, false);
  return xmax(__uint_as_float(sw[0]), __uint_as_float(sw[1]));
}

template <bool SAFE, bool TAIL>
DI f32x16 unit(const s4 (&K0)[4], const s4 (&K1)[4], s4 Q0, s4 Q1, const u4* __restrict__ vl, float kbias, float m) {
  f32x16 ctx = zero16();
#pragma unroll
  for (int kt = 0; kt < 3; ++kt) {
    asm volatile("" ::: "memory");
    f32x16 S = MFMA8(K0[kt], Q0, zero16());
    if (TAIL) S = MFMA8(K1[kt], Q1, S);
#pragma unroll
    for (int i = 0; i < 16; ++i) S[i] = __builtin_amdgcn_exp2f(SAFE ? S[i] - m : S[i]);
    ctx = MFMA16(__builtin_bit_cast(s8, vl[(2 * kt) * 64]), pack8(S, 0), ctx);
    ctx = MFMA16(__builtin_bit_cast(s8, vl[(2 * kt + 1) * 64]), pack8(S, 8), ctx);
  }
  {
    f32x16 S = MFMA8(K0[3], Q0, zero16());
    if (TAIL) S = MFMA8(K1[3], Q1, S);
#pragma unroll
    for (int i = 0; i < 4; ++i) S[i] = __builtin_amdgcn_exp2f(SAFE ? (S[i] + kbias) - m : S[i]);
    u4 pl = {cvt2(S[0], S[1]), cvt2(S[2], S[3]), 0u, 0u};
    ctx = MFMA16(__builtin_bit_cast(s8, vl[6 * 64]), __builtin_bit_cast(s8, pl), ctx);
  }
  return ctx;
}

DI bool bad_denominator(float l) { return !(l >= 8.673617379884035e-19f && l <= 1.152921504606846976e18f); }

DI s8 load_e(const u4* __restrict__ eb, int t, int r, int hh) {
  const bool ok = (t < 3) || (r < 4);
  const u4* p = eb + 64 * t;
  const u4 z = p[ok ? (unsigned)(r * 2 + hh) : 0u];
  const u4 z0 = {0u, 0u, 0u, 0u};
  return __builtin_bit_cast(s8, ok ? z : z0);
}

DI void store_tile(const float (&o)[8], const s8& A2, const s8& Et, uint16_t* __restrict__ ob, int t, int r, int hh, bool pred) {
  const f32x16 p = MFMA16(A2, Et, zero16());
  unsigned a0 = cvt2(o[0] + p[8], o[1] + p[9]);
  unsigned a1 = cvt2(o[2] + p[10], o[3] + p[11]);
  unsigned b0 = cvt2(o[4] + p[12], o[5] + p[13]);
  unsigned b1 = cvt2(o[6] + p[14], o[7] + p[15]);
  u2 s0 = __builtin_amdgcn_permlane32_swap(a0, b0, false, false);
  u2 s1 = __builtin_amdgcn_permlane32_swap(a1, b1, false, false);
  u4 w;
  w[0] = relu2(s0[0]);
  w[1] = relu2(s1[0]);
  w[2] = relu2(s0[1]);
  w[3] = relu2(s1[1]);
  u4* p_out = reinterpret_cast<u4*>(ob) + 64 * t;
  if (pred) p_out[r * 2 + hh] = w;
}

DI void max_pass(const u4* __restrict__ eb, const s4 (&Kf)[2][4], s8 A2, float kbias, int r, int hh, float* __restrict__ ml) {
  const float qscale = 0.35355339059327373f * 1.4426950408889634f;
#pragma unroll 1
  for (int t = 0; t < 4; ++t) {
    const s8 Et = load_e(eb, t, r, hh);
    const f32x16 p = MFMA16(A2, Et, zero16());
    const s4 Q0 = pack4(p[0] * qscale, p[1] * qscale, p[2] * qscale, p[3] * qscale);
    const s4 Q1 = pack4(p[4] * qscale, p[5] * qscale, p[6] * qscale, p[7] * qscale);
    if (t < 3) {
      ml[(2 * t) * 64] = unit_max<false>(Kf[0], Kf[0], Q0, Q0, kbias);
      ml[(2 * t + 1) * 64] = unit_max<false>(Kf[1], Kf[1], Q1, Q1, kbias);
    } else {
      u2 q1 = __builtin_bit_cast(u2, Q1);
      q1[0] = dpp_shr4(q1[0]);
      q1[1] = dpp_shr4(q1[1]);
      ml[6 * 64] = unit_max<true>(Kf[0], Kf[1], Q0, __builtin_bit_cast(s4, q1), kbias);
    }
  }
}

template <bool SAFE>
DI bool process(const u4* __restrict__ eb, const s4 (&Kf)[2][4], const u4* __restrict__ Vf, s8 A2, float kbias, int r,
                int hh, uint16_t* __restrict__ ob, const float* __restrict__ ml) {
  const float qscale = 0.35355339059327373f * 1.4426950408889634f;
  bool bad = false;
  s8 Enext = load_e(eb, 0, r, hh);
#pragma unroll 1
  for (int t = 0; t < 3; ++t) {
    const s8 Et = Enext;
    Enext = load_e(eb, t + 1, r, hh);
    s4 Q0, Q1;
    {
      const f32x16 p = MFMA16(A2, Et, zero16());
      Q0 = pack4(p[0] * qscale, p[1] * qscale, p[2] * qscale, p[3] * qscale);
      Q1 = pack4(p[4] * qscale, p[5] * qscale, p[6] * qscale, p[7] * qscale);
    }
    float o[8];
#pragma unroll
    for (int h = 0; h < 2; ++h) {
      const f32x16 ctx = unit<SAFE, false>(Kf[h], Kf[h], h ? Q1 : Q0, Q0, Vf, kbias, SAFE ? ml[(2 * t + h) * 64] : 0.f);
      bad |= bad_denominator(ctx[8]);
      const float inv = __builtin_amdgcn_rcpf(ctx[8]);
#pragma unroll
      for (int i = 0; i < 4; ++i) o[4 * h + i] = ctx[4 * h + i] * inv;
    }
    store_tile(o, A2, Et, ob, t, r, hh, true);
  }
  {
    const s8 Et = Enext;
    s4 Q0, Q1;
    {
      const f32x16 p = MFMA16(A2, Et, zero16());
      Q0 = pack4(p[0] * qscale, p[1] * qscale, p[2] * qscale, p[3] * qscale);
      Q1 = pack4(p[4] * qscale, p[5] * qscale, p[6] * qscale, p[7] * qscale);
    }
    u2 q1 = __builtin_bit_cast(u2, Q1);
    q1[0] = dpp_shr4(q1[0]);
    q1[1] = dpp_shr4(q1[1]);
    const f32x16 ctx = unit<SAFE, true>(Kf[0], Kf[1], Q0, __builtin_bit_cast(s4, q1), Vf, kbias, SAFE ? ml[6 * 64] : 0.f);
    bad |= bad_denominator(ctx[8]);
    const float inv = __builtin_amdgcn_rcpf(ctx[8]);
    float o[8];
#pragma unroll
    for (int i = 0; i < 4; ++i) {
      o[i] = ctx[i] * inv;
      o[4 + i] = dpp_shl4(ctx[4 + i] * inv);
    }
    store_tile(o, A2, Et, ob, 3, r, hh, r < 4);
  }
  return bad;
}

__global__ __launch_bounds__(1024, 4) void autoint_kernel(const uint16_t* __restrict__ e, const uint16_t* __restrict__ wq,
                                                         const uint16_t* __restrict__ wk, const uint16_t* __restrict__ wv,
                                                         const uint16_t* __restrict__ wres, uint16_t* __restrict__ out,
                                                         int B) {
  const int lane = threadIdx.x & 63;
  const int wv_id = __builtin_amdgcn_readfirstlane(threadIdx.x >> 6);
  const int b = blockIdx.x * 16 + wv_id;
  if (wv_id >= 8) __builtin_amdgcn_s_sleep(80);
  if (b >= B) return;
  const int r = lane & 31, hh = lane >> 5;

  const u4* eb = reinterpret_cast<const u4*>(e + (size_t)b * (F * D));
  s8 E[4];
#pragma unroll
  for (int t = 0; t < 4; ++t) E[t] = load_e(eb, t, r, hh);

  s8 Ak, A2, Bv;
  {
    const int c16 = r & 15;
    const unsigned off_h = (unsigned)((c16 >> 3) * (D * DH) + (c16 & 7) + hh * 8 * DH);
    const unsigned off_r = (unsigned)(c16 + hh * 8 * D);
    s8 fq, fr;
#pragma unroll
    for (int j = 0; j < 8; ++j) {
      Ak[j] = (short)wk[off_h + j * DH];
      Bv[j] = (short)wv[off_h + j * DH];
      fq[j] = (short)wq[off_h + j * DH];
      fr[j] = (short)wres[off_r + j * D];
    }
    const u4 z4 = {0u, 0u, 0u, 0u};
    const bool lo = r < 16;
    Ak = __builtin_bit_cast(s8, lo ? __builtin_bit_cast(u4, Ak) : z4);
    Bv = __builtin_bit_cast(s8, lo ? __builtin_bit_cast(u4, Bv) : z4);
    A2 = __builtin_bit_cast(s8, lo ? __builtin_bit_cast(u4, fq) : __builtin_bit_cast(u4, fr));
  }

  s4 Kf[2][4];
  __shared__ u4 vlds[16 * 7 * 64];
  u4* Vf = vlds + wv_id * (7 * 64) + lane;
  {
    const float one = (r == 16 || r == 20) ? 1.0f : 0.0f;
#pragma unroll
    for (int t = 0; t < 4; ++t) {
      const f32x16 p = MFMA16(Ak, E[t], zero16());
      Kf[0][t] = pack4(p[0], p[1], p[2], p[3]);
      Kf[1][t] = pack4(p[4], p[5], p[6], p[7]);
      f32x16 ci;
#pragma unroll
      for (int i = 0; i < 16; ++i) ci[i] = (t < 3 || (i < 4 && hh == 0)) ? one : 0.f;
      const f32x16 pv = MFMA16(E[t], Bv, ci);
      Vf[(2 * t) * 64] = __builtin_bit_cast(u4, pack8(pv, 0));
      if (t < 3) Vf[(2 * t + 1) * 64] = __builtin_bit_cast(u4, pack8(pv, 8));
    }
  }

  asm volatile("" ::: "memory");
  const float kbias = hh ? -__builtin_inff() : 0.0f;
  uint16_t* ob = out + (size_t)b * (F * D);

  __shared__ float mlds[16 * 7 * 64];
  float* ml = mlds + wv_id * (7 * 64) + lane;
  const bool bad = process<false>(eb, Kf, Vf, A2, kbias, r, hh, ob, ml);
  if (__builtin_amdgcn_ballot_w64(bad) != 0) {
    max_pass(eb, Kf, A2, kbias, r, hh, ml);
    asm volatile("" ::: "memory");
    process<true>(eb, Kf, Vf, A2, kbias, r, hh, ob, ml);
  }
}

extern "C" void kernel_launch(void* const* d_in, const int* in_sizes, int n_in, void* d_out, int out_size, void* d_ws,
                              size_t ws_size, hipStream_t stream) {
  const int B = in_sizes[0] / (F * D);
  autoint_kernel<<<(B + 15) / 16, 1024, 0, stream>>>((const uint16_t*)d_in[0], (const uint16_t*)d_in[1],
                                                  (const uint16_t*)d_in[2], (const uint16_t*)d_in[3],
                                                  (const uint16_t*)d_in[4], (uint16_t*)d_out, B);
}
```

```cpp
#include <hip/hip_runtime.h>
#include <hip/hip_bf16.h>
#include <stdint.h>


typedef float f32x16 __attribute__((ext_vector_type(16)));
typedef float f32x2 __attribute__((ext_vector_type(2)));
typedef __bf16 bf2 __attribute__((ext_vector_type(2)));
typedef short s4 __attribute__((ext_vector_type(4)));
typedef short s8 __attribute__((ext_vector_type(8)));
typedef unsigned u4 __attribute__((ext_vector_type(4)));
typedef unsigned u2 __attribute__((ext_vector_type(2)));
typedef short i16x2 __attribute__((ext_vector_type(2)));

#define DI __device__ __forceinline__

DI unsigned cvt2(float a, float b) {
  f32x2 v = {a, b};
  return __builtin_bit_cast(unsigned, __builtin_convertvector(v, bf2));
}
DI s4 pack4(float a, float b, float c, float d) {
  u2 r = {cvt2(a, b), cvt2(c, d)};
  return __builtin_bit_cast(s4, r);
}
DI s8 pack8(const f32x16& x, int o) {
  u4 r = {cvt2(x[o], x[o + 1]), cvt2(x[o + 2], x[o + 3]), cvt2(x[o + 4], x[o + 5]), cvt2(x[o + 6], x[o + 7])};
  return __builtin_bit_cast(s8, r);
}
DI unsigned relu2(unsigned x) {
  i16x2 v = __builtin_bit_cast(i16x2, x);
  const i16x2 z2 = {0, 0};
  v = __builtin_elementwise_max(v, z2);
  return __builtin_bit_cast(unsigned, v);
}
DI float xmax(float a, float b) { return __builtin_fmaxf(a, b); }
DI float max3(float a, float b, float c) { return __builtin_fmaxf(__builtin_fmaxf(a, b), c); }
DI unsigned dpp_shr4(unsigned x) { return __builtin_amdgcn_update_dpp(0u, x, 0x114, 0xf, 0xf, true); }
DI float dpp_shl4(float x) {
  return __uint_as_float(__builtin_amdgcn_update_dpp(0u, __float_as_uint(x), 0x104, 0xf, 0xf, true));
}

#define MFMA16(a, b, c) __builtin_amdgcn_mfma_f32_32x32x16_bf16((a), (b), (c), 0, 0, 0)
#define MFMA8(a, b, c) __builtin_amdgcn_mfma_f32_32x32x8bf16_1k((a), (b), (c), 0, 0, 0)

constexpr int F = 100, D = 16, DH = 8;

DI f32x16 zero16() {
  f32x16 z;
#pragma unroll
  for (int i = 0; i < 16; ++i) z[i] = 0.f;
  return z;
}

template <bool TAIL>
DI float unit_max(const s4 (&K0)[4], const s4 (&K1)[4], s4 Q0, s4 Q1, float kbias) {
  float m = -__builtin_inff();
#pragma unroll
  for (int kt = 0; kt < 4; ++kt) {
    f32x16 S = MFMA8(K0[kt], Q0, zero16());
    if (TAIL) S = MFMA8(K1[kt], Q1, S);
    if (kt < 3) {
#pragma unroll
      for (int i = 0; i < 16; i += 2) m = max3(m, S[i], S[i + 1]);
    } else {
      m = max3(m, S[0] + kbias, S[1] + kbias);
      m = max3(m, S[2] + kbias, S[3] + kbias);
    }
  }
  auto sw = __builtin_amdgcn_permlane32_swap(__float_as_uint(m), __float_as_uint(m), false, false);
  return xmax(__uint_as_float(sw[0]), __uint_as_float(sw[1]));
}

template <bool SAFE, bool TAIL>
DI f32x16 unit(const s4 (&K0)[4], const s4 (&K1)[4], s4 Q0, s4 Q1, const u4* __restrict__ vl, float kbias, float m) {
  f32x16 ctx = zero16();
#pragma unroll
  for (int kt = 0; kt < 3; ++kt) {
    asm volatile("" ::: "memory");
    f32x16 S = MFMA8(K0[kt], Q0, zero16());
    if (TAIL) S = MFMA8(K1[kt], Q1, S);
#pragma unroll
    for (int i = 0; i < 16; ++i) S[i] = __builtin_amdgcn_exp2f(SAFE ? S[i] - m : S[i]);
    ctx = MFMA16(__builtin_bit_cast(s8, vl[(2 * kt) * 64]), pack8(S, 0), ctx);
    ctx = MFMA16(__builtin_bit_cast(s8, vl[(2 * kt + 1) * 64]), pack8(S, 8), ctx);
  }
  {
    f32x16 S = MFMA8(K0[3], Q0, zero16());
    if (TAIL) S = MFMA8(K1[3], Q1, S);
#pragma unroll
    for (int i = 0; i < 4; ++i) S[i] = __builtin_amdgcn_exp2f(SAFE ? (S[i] + kbias) - m : S[i]);
    u4 pl = {cvt2(S[0], S[1]), cvt2(S[2], S[3]), 0u, 0u};
    ctx = MFMA16(__builtin_bit_cast(s8, vl[6 * 64]), __builtin_bit_cast(s8, pl), ctx);
  }
  return ctx;
}

DI bool bad_denominator(float l) { return !(l >= 8.673617379884035e-19f && l <= 1.152921504606846976e18f); }

DI s8 load_e(const u4* __restrict__ eb, int t, int r, int hh) {
  const bool ok = (t < 3) || (r < 4);
  const u4* p = eb + 64 * t;
  const u4 z = p[ok ? (unsigned)(r * 2 + hh) : 0u];
  const u4 z0 = {0u, 0u, 0u, 0u};
  return __builtin_bit_cast(s8, ok ? z : z0);
}

DI void store_tile(const float (&o)[8], const s8& A2, const s8& Et, uint16_t* __restrict__ ob, int t, int r, int hh, bool pred) {
  const f32x16 p = MFMA16(A2, Et, zero16());
  unsigned a0 = cvt2(o[0] + p[8], o[1] + p[9]);
  unsigned a1 = cvt2(o[2] + p[10], o[3] + p[11]);
  unsigned b0 = cvt2(o[4] + p[12], o[5] + p[13]);
  unsigned b1 = cvt2(o[6] + p[14], o[7] + p[15]);
  u2 s0 = __builtin_amdgcn_permlane32_swap(a0, b0, false, false);
  u2 s1 = __builtin_amdgcn_permlane32_swap(a1, b1, false, false);
  u4 w;
  w[0] = relu2(s0[0]);
  w[1] = relu2(s1[0]);
  w[2] = relu2(s0[1]);
  w[3] = relu2(s1[1]);
  u4* p_out = reinterpret_cast<u4*>(ob) + 64 * t;
  if (pred) p_out[r * 2 + hh] = w;
}

DI void max_pass(const u4* __restrict__ eb, const s4 (&Kf)[2][4], s8 A2, float kbias, int r, int hh, float* __restrict__ ml) {
  const float qscale = 0.35355339059327373f * 1.4426950408889634f;
#pragma unroll 1
  for (int t = 0; t < 4; ++t) {
    const s8 Et = load_e(eb, t, r, hh);
    const f32x16 p = MFMA16(A2, Et, zero16());
    const s4 Q0 = pack4(p[0] * qscale, p[1] * qscale, p[2] * qscale, p[3] * qscale);
    const s4 Q1 = pack4(p[4] * qscale, p[5] * qscale, p[6] * qscale, p[7] * qscale);
    if (t < 3) {
      ml[(2 * t) * 64] = unit_max<false>(Kf[0], Kf[0], Q0, Q0, kbias);
      ml[(2 * t + 1) * 64] = unit_max<false>(Kf[1], Kf[1], Q1, Q1, kbias);
    } else {
      u2 q1 = __builtin_bit_cast(u2, Q1);
      q1[0] = dpp_shr4(q1[0]);
      q1[1] = dpp_shr4(q1[1]);
      ml[6 * 64] = unit_max<true>(Kf[0], Kf[1], Q0, __builtin_bit_cast(s4, q1), kbias);
    }
  }
}

template <bool SAFE>
DI bool process(const u4* __restrict__ eb, const s4 (&Kf)[2][4], const u4* __restrict__ Vf, s8 A2, float kbias, int r,
                int hh, uint16_t* __restrict__ ob, const float* __restrict__ ml) {
  const float qscale = 0.35355339059327373f * 1.4426950408889634f;
  bool bad = false;
  s8 Enext = load_e(eb, 0, r, hh);
#pragma unroll 1
  for (int t = 0; t < 3; ++t) {
    const s8 Et = Enext;
    Enext = load_e(eb, t + 1, r, hh);
    s4 Q0, Q1;
    {
      const f32x16 p = MFMA16(A2, Et, zero16());
      Q0 = pack4(p[0] * qscale, p[1] * qscale, p[2] * qscale, p[3] * qscale);
      Q1 = pack4(p[4] * qscale, p[5] * qscale, p[6] * qscale, p[7] * qscale);
    }
    float o[8];
#pragma unroll
    for (int h = 0; h < 2; ++h) {
      const f32x16 ctx = unit<SAFE, false>(Kf[h], Kf[h], h ? Q1 : Q0, Q0, Vf, kbias, SAFE ? ml[(2 * t + h) * 64] : 0.f);
      bad |= bad_denominator(ctx[8]);
      const float inv = __builtin_amdgcn_rcpf(ctx[8]);
#pragma unroll
      for (int i = 0; i < 4; ++i) o[4 * h + i] = ctx[4 * h + i] * inv;
    }
    store_tile(o, A2, Et, ob, t, r, hh, true);
    if (t == 0) __builtin_amdgcn_s_setprio(2);
    else if (t == 1) __builtin_amdgcn_s_setprio(1);
    else __builtin_amdgcn_s_setprio(0);
  }
  {
    const s8 Et = Enext;
    s4 Q0, Q1;
    {
      const f32x16 p = MFMA16(A2, Et, zero16());
      Q0 = pack4(p[0] * qscale, p[1] * qscale, p[2] * qscale, p[3] * qscale);
      Q1 = pack4(p[4] * qscale, p[5] * qscale, p[6] * qscale, p[7] * qscale);
    }
    u2 q1 = __builtin_bit_cast(u2, Q1);
    q1[0] = dpp_shr4(q1[0]);
    q1[1] = dpp_shr4(q1[1]);
    const f32x16 ctx = unit<SAFE, true>(Kf[0], Kf[1], Q0, __builtin_bit_cast(s4, q1), Vf, kbias, SAFE ? ml[6 * 64] : 0.f);
    bad |= bad_denominator(ctx[8]);
    const float inv = __builtin_amdgcn_rcpf(ctx[8]);
    float o[8];
#pragma unroll
    for (int i = 0; i < 4; ++i) {
      o[i] = ctx[i] * inv;
      o[4 + i] = dpp_shl4(ctx[4 + i] * inv);
    }
    store_tile(o, A2, Et, ob, 3, r, hh, r < 4);
  }
  return bad;
}

__global__ __launch_bounds__(256, 4) void autoint_kernel(const uint16_t* __restrict__ e, const uint16_t* __restrict__ wq,
                                                         const uint16_t* __restrict__ wk, const uint16_t* __restrict__ wv,
                                                         const uint16_t* __restrict__ wres, uint16_t* __restrict__ out,
                                                         int B) {
  __builtin_amdgcn_s_setprio(3);
  const int lane = threadIdx.x & 63;
  const int wv_id = __builtin_amdgcn_readfirstlane(threadIdx.x >> 6);
  const int b = blockIdx.x * 4 + wv_id;
  if (b >= B) return;
  const int r = lane & 31, hh = lane >> 5;

  const u4* eb = reinterpret_cast<const u4*>(e + (size_t)b * (F * D));
  s8 E[4];
#pragma unroll
  for (int t = 0; t < 4; ++t) E[t] = load_e(eb, t, r, hh);

  s8 Ak, A2, Bv;
  {
    const int c16 = r & 15;
    const unsigned off_h = (unsigned)((c16 >> 3) * (D * DH) + (c16 & 7) + hh * 8 * DH);
    const unsigned off_r = (unsigned)(c16 + hh * 8 * D);
    s8 fq, fr;
#pragma unroll
    for (int j = 0; j < 8; ++j) {
      Ak[j] = (short)wk[off_h + j * DH];
      Bv[j] = (short)wv[off_h + j * DH];
      fq[j] = (short)wq[off_h + j * DH];
      fr[j] = (short)wres[off_r + j * D];
    }
    const u4 z4 = {0u, 0u, 0u, 0u};
    const bool lo = r < 16;
    Ak = __builtin_bit_cast(s8, lo ? __builtin_bit_cast(u4, Ak) : z4);
    Bv = __builtin_bit_cast(s8, lo ? __builtin_bit_cast(u4, Bv) : z4);
    A2 = __builtin_bit_cast(s8, lo ? __builtin_bit_cast(u4, fq) : __builtin_bit_cast(u4, fr));
  }

  s4 Kf[2][4];
  __shared__ u4 vlds[4 * 7 * 64];
  u4* Vf = vlds + wv_id * (7 * 64) + lane;
  {
    const float one = (r == 16 || r == 20) ? 1.0f : 0.0f;
#pragma unroll
    for (int t = 0; t < 4; ++t) {
      const f32x16 p = MFMA16(Ak, E[t], zero16());
      Kf[0][t] = pack4(p[0], p[1], p[2], p[3]);
      Kf[1][t] = pack4(p[4], p[5], p[6], p[7]);
      f32x16 ci;
#pragma unroll
      for (int i = 0; i < 16; ++i) ci[i] = (t < 3 || (i < 4 && hh == 0)) ? one : 0.f;
      const f32x16 pv = MFMA16(E[t], Bv, ci);
      Vf[(2 * t) * 64] = __builtin_bit_cast(u4, pack8(pv, 0));
      if (t < 3) Vf[(2 * t + 1) * 64] = __builtin_bit_cast(u4, pack8(pv, 8));
    }
  }

  asm volatile("" ::: "memory");
  const float kbias = hh ? -__builtin_inff() : 0.0f;
  uint16_t* ob = out + (size_t)b * (F * D);

  __shared__ float mlds[4 * 7 * 64];
  float* ml = mlds + wv_id * (7 * 64) + lane;
  const bool bad = process<false>(eb, Kf, Vf, A2, kbias, r, hh, ob, ml);
  if (__builtin_amdgcn_ballot_w64(bad) != 0) {
    max_pass(eb, Kf, A2, kbias, r, hh, ml);
    asm volatile("" ::: "memory");
    process<true>(eb, Kf, Vf, A2, kbias, r, hh, ob, ml);
  }
}

extern "C" void kernel_launch(void* const* d_in, const int* in_sizes, int n_in, void* d_out, int out_size, void* d_ws,
                              size_t ws_size, hipStream_t stream) {
  const int B = in_sizes[0] / (F * D);
  autoint_kernel<<<(B + 3) / 4, 256, 0, stream>>>((const uint16_t*)d_in[0], (const uint16_t*)d_in[1],
                                                  (const uint16_t*)d_in[2], (const uint16_t*)d_in[3],
                                                  (const uint16_t*)d_in[4], (uint16_t*)d_out, B);
}
```
